```python
import torch

def _softmax(x, axis=-1):
    return torch.softmax(x, dim=axis)
def _gelu(x):
    return torch.nn.functional.gelu(x, approximate="tanh")
def _silu(x):
    return torch.nn.functional.silu(x)
def _sigmoid(x):
    return torch.sigmoid(x)
def _layernorm(x, axis=-1, eps=1e-5):
    m = x.mean(dim=axis, keepdim=True)
    v = x.var(dim=axis, keepdim=True, unbiased=False)
    return (x - m) / torch.sqrt(v + eps)
def _scatter_add(tgt, idx, src, axis=0):
    return tgt.clone().index_add_(axis, idx, src)
def _segment_sum(src, seg, n):
    out = torch.zeros((n,) + src.shape[1:], dtype=src.dtype)
    return out.index_add_(0, seg, src)
def _pad(x, pw):
    flat = tuple(v for pair in reversed(pw) for v in pair)
    return torch.nn.functional.pad(x, flat)

kernel_name = 'parallel_sum__sliding_block__glu_gated_reglu__prompt'

def setup_inputs():

    _w = (lambda a: (lambda t: t.to(torch.bfloat16) if t.is_floating_point() else t)(torch.as_tensor(a)))
    def _A_setup():
        B, S, D = 1, 2048, 1024
        return {
            "x": _w((lambda *s: torch.randn(*s, dtype=torch.bfloat16) * 0.05)(B, S, D)),
            "wqkv": _w((lambda *s: torch.randn(*s, dtype=torch.bfloat16) * 0.05)(D, 3 * D)),
            "wo": _w((lambda *s: torch.randn(*s, dtype=torch.bfloat16) * 0.05)(D, D)),
        }
    _A_in = _A_setup()

    def _B_setup():
        B, S, D, F = 1, 2048, 1024, 3584
        return {
            "x": _w((lambda *s: torch.randn(*s, dtype=torch.bfloat16) * 0.05)(B, S, D)),
            "W_gate": _w((lambda *s: torch.randn(*s, dtype=torch.bfloat16) * 0.8)(D, F) * (2.0 / D) ** 0.5),
            "W_up": _w((lambda *s: torch.randn(*s, dtype=torch.bfloat16) * 0.8)(D, F) * (2.0 / D) ** 0.5),
            "W_down": _w((lambda *s: torch.randn(*s, dtype=torch.bfloat16) * 0.8)(F, D) * (1.0 / F) ** 0.5),
        }
    _B_in = _B_setup()

    return {
        "A_x": _A_in["x"],
        "A_wqkv": _A_in["wqkv"],
        "A_wo": _A_in["wo"],
        "B_x": _B_in["x"],
        "B_W_gate": _B_in["W_gate"],
        "B_W_up": _B_in["W_up"],
        "B_W_down": _B_in["W_down"],
    }

def reference(A_x, A_wqkv, A_wo, B_x, B_W_gate, B_W_up, B_W_down):
    def _A_ref(x, wqkv, wo):
        B, S, D = x.shape
        H = 32; Dh = D // H; W = 256
        qkv = (lambda x, *d: x.permute(*d))((x @ wqkv).reshape(B, S, 3, H, Dh), 2, 0, 3, 1, 4)
        q, k, v = qkv[0], qkv[1], qkv[2]
        scores = torch.einsum("bhqd,bhkd->bhqk", q, k) * (Dh ** -0.5)
        idx = torch.arange(S)
        mask = (idx[:, None] - idx[None, :] >= 0) & (idx[:, None] - idx[None, :] < W)
        scores = torch.where(mask[None, None], scores, -1e9)
        p = _softmax(scores, axis=-1)
        o = torch.einsum("bhqk,bhkd->bhqd", p, v)
        return (lambda x, *d: x.permute(*d))(o, 0, 2, 1, 3).reshape(B, S, D) @ wo

    def _B_ref(x, W_gate, W_up, W_down):
        gate = torch.einsum("bsd,df->bsf", x, W_gate)
        up = torch.einsum("bsd,df->bsf", x, W_up)
        gate = gate * (lambda x, dt: x.to(dt))(gate > 0, gate.dtype)
        h = gate * up
        return torch.einsum("bsf,fd->bsd", h, W_down)

    x = A_x
    xn = _layernorm(x, axis=-1)
    return x + _A_ref(xn, A_wqkv, A_wo) + _B_ref(xn, B_W_gate, B_W_up, B_W_down)
```

```cpp
#include <hip/hip_runtime.h>
#include <hip/hip_bf16.h>
#include <stdint.h>

typedef __attribute__((ext_vector_type(8))) short bf16x8;
typedef __attribute__((ext_vector_type(4))) short s16x4;
typedef __attribute__((ext_vector_type(4))) float f32x4;
typedef __attribute__((ext_vector_type(16))) float f32x16;
typedef __attribute__((ext_vector_type(4))) unsigned u32x4;
typedef __attribute__((ext_vector_type(2))) unsigned u32x2;
typedef unsigned short u16;

constexpr int S_ = 2048, D_ = 1024, F_ = 3584, H_ = 32, DH_ = 32, WIN_ = 256;
constexpr int K2_ = D_ + F_;

#define DI __device__ __forceinline__

DI float bf2f(u16 v) { return __uint_as_float((unsigned)v << 16); }
DI u16 f2bf(float f) { __hip_bfloat16 h = __float2bfloat16(f); return __bfloat16_as_ushort(h); }
DI float rbf(float f) { return bf2f(f2bf(f)); }
DI unsigned pack2(float a, float b) { return (unsigned)f2bf(a) | ((unsigned)f2bf(b) << 16); }

__global__ __launch_bounds__(256) void ln_kernel(const u16* __restrict__ x, u16* __restrict__ xn) {
  const int wave = threadIdx.x >> 6, lane = threadIdx.x & 63;
  const int row = blockIdx.x * 4 + wave;
  const u16* xr = x + (size_t)row * D_;
  bf16x8 v0 = *reinterpret_cast<const bf16x8*>(xr + lane * 8);
  bf16x8 v1 = *reinterpret_cast<const bf16x8*>(xr + 512 + lane * 8);
  float f[16];
#pragma unroll
  for (int j = 0; j < 8; ++j) { f[j] = bf2f((u16)v0[j]); f[8 + j] = bf2f((u16)v1[j]); }
  float s = 0.f;
#pragma unroll
  for (int j = 0; j < 16; ++j) s += f[j];
#pragma unroll
  for (int o = 32; o > 0; o >>= 1) s += __shfl_xor(s, o);
  const float mean = s * (1.0f / D_);
  float q = 0.f;
#pragma unroll
  for (int j = 0; j < 16; ++j) { float d = f[j] - mean; q += d * d; }
#pragma unroll
  for (int o = 32; o > 0; o >>= 1) q += __shfl_xor(q, o);
  const float var = q * (1.0f / D_);
  const float mb = rbf(mean);
  const float vb = rbf(var);
  const float sd = rbf(sqrtf(rbf(vb + 1e-5f)));
  u32x4 o0, o1;
#pragma unroll
  for (int j = 0; j < 4; ++j) {
    o0[j] = pack2(rbf(f[2 * j] - mb) / sd, rbf(f[2 * j + 1] - mb) / sd);
    o1[j] = pack2(rbf(f[8 + 2 * j] - mb) / sd, rbf(f[8 + 2 * j + 1] - mb) / sd);
  }
  u16* yr = xn + (size_t)row * D_;
  *reinterpret_cast<u32x4*>(yr + lane * 8) = o0;
  *reinterpret_cast<u32x4*>(yr + 512 + lane * 8) = o1;
}

DI int a_lds_byte(int r, int c) {
  int st = (r >> 4) * 2 + (c >> 5), rr = r & 15, cc = c & 31, ob = rr * 64 + cc * 2;
  return st * 1024 + (ob ^ (((ob >> 9) & 1) << 5));
}

#define WAIT_V(n) asm volatile("s_waitcnt vmcnt(" #n ")" ::: "memory")
#define WAIT_L(n) asm volatile("s_waitcnt lgkmcnt(" #n ")" ::: "memory")
#define BAR __builtin_amdgcn_s_barrier()

DI s16x4 tr_read(const char* p) {
  return __builtin_amdgcn_ds_read_tr16_b64_v4i16((__attribute__((address_space(3))) s16x4*)(p));
}

constexpr int G_STAGE = 32768;
constexpr int G_NS = 3;

template <int MODE>
__global__ __launch_bounds__(256, 1) void gemm128_kernel(
    const u16* __restrict__ A, const u16* __restrict__ B0, const u16* __restrict__ B1, const u16* __restrict__ B2,
    u16* __restrict__ o_qk, u16* __restrict__ o_vt, u16* __restrict__ o_a2, float* __restrict__ o_part, int tile_base) {
  extern __shared__ __attribute__((aligned(16))) char shm[];
  const int T = threadIdx.x, wid = T >> 6, lane = T & 63, wr = wid >> 1, wc = wid & 1, fr = lane & 15, fq = lane >> 4;

  int mt, nt, kbeg, nkt, lda;
  if (MODE == 0) {
    const int tile = blockIdx.x + tile_base;
    mt = tile & 15; nt = tile >> 4; kbeg = 0; nkt = D_ / 64; lda = D_;
  } else {
    const int tile = blockIdx.x;
    mt = tile & 15; nt = (tile >> 4) & 7; kbeg = (tile >> 7) * (K2_ / 2); nkt = K2_ / 2 / 64; lda = K2_;
  }
  const int m0 = mt * 128;

  const int sb = (T & 63) * 16, swz = sb ^ (((sb >> 9) & 1) << 5);
  const u16* a_src = A + (size_t)(m0 + (T >> 7) * 16 + (swz >> 6)) * lda + kbeg + ((T >> 6) & 1) * 32 + ((swz & 63) >> 1);
  const int nb = (T >> 4) & 7, br = ((T & 15) >> 1) ^ (((T >> 7) & 1) * 4), hc = T & 1;
  const int brow = (T >> 7) * 8 + br;
  const u16* b_src;
  int ldb;
  bool is_gu = false, is_v = false;
  if (MODE == 0) {
    if (nt < 24) { b_src = B0 + (size_t)brow * 3072 + nt * 128 + nb * 16 + hc * 8; ldb = 3072; is_v = nt >= 16; }
    else { const int f0 = (nt - 24) * 64; b_src = ((nb & 1) ? B2 : B1) + (size_t)brow * F_ + f0 + (nb >> 1) * 16 + hc * 8; ldb = F_; is_gu = true; }
  } else {
    b_src = nullptr; ldb = D_;
  }

  auto stage = [&](int kt, int slot) {
    char* dst = shm + slot * G_STAGE + T * 16;
    const u16* ap = a_src + kt * 64;
    const u16* bp;
    if (MODE == 0) bp = b_src + (size_t)kt * 64 * ldb;
    else {
      const int kg = kbeg + kt * 64;
      const u16* base = (kg < D_) ? (B0 + (size_t)kg * D_) : (B1 + (size_t)(kg - D_) * D_);
      bp = base + (size_t)brow * D_ + nt * 128 + nb * 16 + hc * 8;
    }
#pragma unroll
    for (int i = 0; i < 4; ++i)
      __builtin_amdgcn_global_load_lds((const unsigned*)(ap + (size_t)i * 32 * lda), (unsigned*)(dst + i * 4096), 16, 0, 0);
#pragma unroll
    for (int i = 0; i < 4; ++i)
      __builtin_amdgcn_global_load_lds((const unsigned*)(bp + (size_t)i * 16 * ldb), (unsigned*)(dst + 16384 + i * 4096), 16, 0, 0);
  };

  f32x4 acc[4][4];
#pragma unroll
  for (int m = 0; m < 4; ++m)
#pragma unroll
    for (int n = 0; n < 4; ++n) acc[m][n] = f32x4{0.f, 0.f, 0.f, 0.f};

  const int q4 = fr >> 2, p4 = fr & 3;
  const int b_off0 = ((fq * 8 + wc * 4) * 256) + ((q4 ^ ((fq & 1) * 4)) * 32) + p4 * 8;
  const int b_off1 = ((fq * 8 + wc * 4) * 256) + (((4 + q4) ^ ((fq & 1) * 4)) * 32) + p4 * 8;

  const bool swap = (MODE == 1) || !is_v;

  stage(0, 0);
  stage(1, 1);
  for (int kt = 0; kt < nkt; ++kt) {
    if (kt + 1 < nkt) { WAIT_V(8); } else { WAIT_V(0); }
    BAR;
    if (kt + 2 < nkt) stage(kt + 2, (kt + 2) % G_NS);
    const char* sa = shm + (kt % G_NS) * G_STAGE;
    const char* sbp = sa + 16384;
#pragma unroll
    for (int ks = 0; ks < 2; ++ks) {
      bf16x8 af[4], bfr[4];
#pragma unroll
      for (int m = 0; m < 4; ++m)
        af[m] = *reinterpret_cast<const bf16x8*>(sa + a_lds_byte(wr * 64 + m * 16 + fr, ks * 32 + fq * 8));
#pragma unroll
      for (int n = 0; n < 4; ++n) {
        s16x4 lo = tr_read(sbp + ks * 8192 + n * 256 + b_off0);
        s16x4 hi = tr_read(sbp + ks * 8192 + n * 256 + b_off1);
        bfr[n] = __builtin_shufflevector(lo, hi, 0, 1, 2, 3, 4, 5, 6, 7);
      }
      if (swap) {
#pragma unroll
        for (int m = 0; m < 4; ++m)
#pragma unroll
          for (int n = 0; n < 4; ++n) acc[m][n] = __builtin_amdgcn_mfma_f32_16x16x32_bf16(bfr[n], af[m], acc[m][n], 0, 0, 0);
      } else {
#pragma unroll
        for (int m = 0; m < 4; ++m)
#pragma unroll
          for (int n = 0; n < 4; ++n) acc[m][n] = __builtin_amdgcn_mfma_f32_16x16x32_bf16(af[m], bfr[n], acc[m][n], 0, 0, 0);
      }
    }
  }

  if (MODE == 0) {
    if (is_gu) {
      const int f0 = (nt - 24) * 64;
#pragma unroll
      for (int m = 0; m < 4; ++m) {
        const int row = m0 + wr * 64 + m * 16 + fr;
#pragma unroll
        for (int n2 = 0; n2 < 2; ++n2) {
          float hv[4];
#pragma unroll
          for (int j = 0; j < 4; ++j) {
            float g = rbf(acc[m][2 * n2][j]), u = rbf(acc[m][2 * n2 + 1][j]);
            hv[j] = (g > 0.f ? g : 0.f) * u;
          }
          u32x2 o; o[0] = pack2(hv[0], hv[1]); o[1] = pack2(hv[2], hv[3]);
          const int col = D_ + f0 + (wc * 2 + n2) * 16 + fq * 4;
          *reinterpret_cast<u32x2*>(o_a2 + (size_t)row * K2_ + col) = o;
        }
      }
    } else if (!is_v) {
#pragma unroll
      for (int m = 0; m < 4; ++m) {
        const int row = m0 + wr * 64 + m * 16 + fr;
#pragma unroll
        for (int n = 0; n < 4; ++n) {
          u32x2 o; o[0] = pack2(acc[m][n][0], acc[m][n][1]); o[1] = pack2(acc[m][n][2], acc[m][n][3]);
          const int col = nt * 128 + wc * 64 + n * 16 + fq * 4;
          *reinterpret_cast<u32x2*>(o_qk + (size_t)row * 2048 + col) = o;
        }
      }
    } else {
#pragma unroll
      for (int m = 0; m < 4; ++m) {
        const int row = m0 + wr * 64 + m * 16 + fq * 4;
#pragma unroll
        for (int n = 0; n < 4; ++n) {
          u32x2 o; o[0] = pack2(acc[m][n][0], acc[m][n][1]); o[1] = pack2(acc[m][n][2], acc[m][n][3]);
          const int col = (nt - 16) * 128 + wc * 64 + n * 16 + fr;
          *reinterpret_cast<u32x2*>(o_vt + (size_t)col * S_ + row) = o;
        }
      }
    }
  } else {
    float* P = o_part + (size_t)(blockIdx.x >> 7) * S_ * D_;
#pragma unroll
    for (int m = 0; m < 4; ++m) {
      const int row = m0 + wr * 64 + m * 16 + fr;
#pragma unroll
      for (int n = 0; n < 4; ++n) {
        const int col = nt * 128 + wc * 64 + n * 16 + fq * 4;
        *reinterpret_cast<f32x4*>(P + (size_t)row * D_ + col) = acc[m][n];
      }
    }
  }
}

__global__ __launch_bounds__(256) void attn_kernel(const u16* __restrict__ qk, const u16* __restrict__ vt, u16* __restrict__ a2) {
  const int wid = threadIdx.x >> 6, lane = threadIdx.x & 63;
  const int item = blockIdx.x * 4 + wid;
  const int head = item >> 6, qb = item & 63;
  const int q0 = qb * 32;
  const int c = lane & 31, h = lane >> 5;
  const u16* qp = qk + (size_t)(q0 + c) * 2048 + head * 32 + h * 8;
  const bf16x8 qf0 = *reinterpret_cast<const bf16x8*>(qp);
  const bf16x8 qf1 = *reinterpret_cast<const bf16x8*>(qp + 16);
  const int tfirst = (q0 >= WIN_) ? 0 : (WIN_ - q0) / 32;

  f32x16 sc[9];
  const float NEG = -1e30f;
#pragma unroll
  for (int t = 0; t < 9; ++t) {
    f32x16 z;
#pragma unroll
    for (int i = 0; i < 16; ++i) z[i] = NEG;
    if (t >= tfirst) {
      const int key0 = q0 - WIN_ + 32 * t;
      const u16* kp = qk + (size_t)(key0 + c) * 2048 + 1024 + head * 32 + h * 8;
      const bf16x8 kf0 = *reinterpret_cast<const bf16x8*>(kp);
      const bf16x8 kf1 = *reinterpret_cast<const bf16x8*>(kp + 16);
#pragma unroll
      for (int i = 0; i < 16; ++i) z[i] = 0.f;
      z = __builtin_amdgcn_mfma_f32_32x32x16_bf16(kf0, qf0, z, 0, 0, 0);
      z = __builtin_amdgcn_mfma_f32_32x32x16_bf16(kf1, qf1, z, 0, 0, 0);
      if (t == 0) {
#pragma unroll
        for (int i = 0; i < 16; ++i) { const int kl = (i & 3) + 8 * (i >> 2) + 4 * h; if (kl <= c) z[i] = NEG; }
      }
      if (t == 8) {
#pragma unroll
        for (int i = 0; i < 16; ++i) { const int kl = (i & 3) + 8 * (i >> 2) + 4 * h; if (kl > c) z[i] = NEG; }
      }
    }
    sc[t] = z;
  }
  float mx = NEG;
#pragma unroll
  for (int t = 0; t < 9; ++t)
#pragma unroll
    for (int i = 0; i < 16; ++i) mx = fmaxf(mx, sc[t][i]);
  mx = fmaxf(mx, __shfl_xor(mx, 32));
  const float cs = 0.17677669529663687f * 1.4426950408889634f;
  float sum = 0.f;
#pragma unroll
  for (int t = 0; t < 9; ++t)
#pragma unroll
    for (int i = 0; i < 16; ++i) { float p = exp2f((sc[t][i] - mx) * cs); sc[t][i] = p; sum += p; }
  sum += __shfl_xor(sum, 32);

  f32x16 o;
#pragma unroll
  for (int i = 0; i < 16; ++i) o[i] = 0.f;
  const u16* vbase = vt + (size_t)(head * 32 + c) * S_;
#pragma unroll
  for (int t = 0; t < 9; ++t) {
    if (t >= tfirst) {
      const int key0 = q0 - WIN_ + 32 * t;
#pragma unroll
      for (int s = 0; s < 2; ++s) {
        const u16* vp = vbase + key0 + 16 * s + 4 * h;
        const s16x4 lo = *reinterpret_cast<const s16x4*>(vp);
        const s16x4 hi = *reinterpret_cast<const s16x4*>(vp + 8);
        const bf16x8 vf = __builtin_shufflevector(lo, hi, 0, 1, 2, 3, 4, 5, 6, 7);
        u32x4 pk;
#pragma unroll
        for (int j = 0; j < 4; ++j) pk[j] = pack2(sc[t][8 * s + 2 * j], sc[t][8 * s + 2 * j + 1]);
        const bf16x8 pf = __builtin_bit_cast(bf16x8, pk);
        o = __builtin_amdgcn_mfma_f32_32x32x16_bf16(vf, pf, o, 0, 0, 0);
      }
    }
  }
  const float inv = 1.0f / sum;
  u16* op = a2 + (size_t)(q0 + c) * K2_ + head * 32 + 4 * h;
#pragma unroll
  for (int g = 0; g < 4; ++g) {
    u32x2 w; w[0] = pack2(o[4 * g] * inv, o[4 * g + 1] * inv); w[1] = pack2(o[4 * g + 2] * inv, o[4 * g + 3] * inv);
    *reinterpret_cast<u32x2*>(op + 8 * g) = w;
  }
}

__global__ __launch_bounds__(256) void reduce_kernel(const u16* __restrict__ x, const float* __restrict__ part, u16* __restrict__ out) {
  const size_t i = ((size_t)blockIdx.x * 256 + threadIdx.x) * 8;
  const bf16x8 xv = *reinterpret_cast<const bf16x8*>(x + i);
  const f32x4 a0 = *reinterpret_cast<const f32x4*>(part + i), a1 = *reinterpret_cast<const f32x4*>(part + i + 4);
  const f32x4 b0 = *reinterpret_cast<const f32x4*>(part + (size_t)S_ * D_ + i), b1 = *reinterpret_cast<const f32x4*>(part + (size_t)S_ * D_ + i + 4);
  u32x4 o;
#pragma unroll
  for (int j = 0; j < 2; ++j) {
    o[j] = pack2(bf2f((u16)xv[2 * j]) + a0[2 * j] + b0[2 * j], bf2f((u16)xv[2 * j + 1]) + a0[2 * j + 1] + b0[2 * j + 1]);
    o[2 + j] = pack2(bf2f((u16)xv[4 + 2 * j]) + a1[2 * j] + b1[2 * j], bf2f((u16)xv[4 + 2 * j + 1]) + a1[2 * j + 1] + b1[2 * j + 1]);
  }
  *reinterpret_cast<u32x4*>(out + i) = o;
}

extern "C" void kernel_launch(void* const* d_in, const int* in_sizes, int n_in, void* d_out, int out_size, void* d_ws,
                              size_t ws_size, hipStream_t stream) {
  const u16* x = (const u16*)d_in[0];
  const u16* wqkv = (const u16*)d_in[1];
  const u16* wo = (const u16*)d_in[2];
  const u16* wg = (const u16*)d_in[4];
  const u16* wu = (const u16*)d_in[5];
  const u16* wd = (const u16*)d_in[6];
  char* ws = (char*)d_ws;
  u16* xn = (u16*)(ws);
  u16* qk = (u16*)(ws + (4u << 20));
  u16* vt = (u16*)(ws + (12u << 20));
  u16* a2 = (u16*)(ws + (16u << 20));
  float* part = (float*)(ws + (36u << 20));

  hipFuncSetAttribute((const void*)gemm128_kernel<0>, hipFuncAttributeMaxDynamicSharedMemorySize, G_NS * G_STAGE);
  hipFuncSetAttribute((const void*)gemm128_kernel<1>, hipFuncAttributeMaxDynamicSharedMemorySize, G_NS * G_STAGE);

  ln_kernel<<<S_ / 4, 256, 0, stream>>>(x, xn);
  gemm128_kernel<0><<<16 * 80, 256, G_NS * G_STAGE, stream>>>(xn, wqkv, wg, wu, qk, vt, a2, nullptr, 0);
  attn_kernel<<<2048 / 4, 256, 0, stream>>>(qk, vt, a2);
  gemm128_kernel<1><<<256, 256, G_NS * G_STAGE, stream>>>(a2, wo, wd, nullptr, nullptr, nullptr, nullptr, part, 0);
  reduce_kernel<<<S_ * D_ / 8 / 256, 256, 0, stream>>>(x, part, (u16*)d_out);
}
```

```cpp
#include <hip/hip_runtime.h>
#include <hip/hip_bf16.h>
#include <stdint.h>

typedef __attribute__((ext_vector_type(8))) short bf16x8;
typedef __attribute__((ext_vector_type(4))) short s16x4;
typedef __attribute__((ext_vector_type(4))) float f32x4;
typedef __attribute__((ext_vector_type(16))) float f32x16;
typedef __attribute__((ext_vector_type(4))) unsigned u32x4;
typedef __attribute__((ext_vector_type(2))) unsigned u32x2;
typedef unsigned short u16;

constexpr int S_ = 2048, D_ = 1024, F_ = 3584, H_ = 32, DH_ = 32, WIN_ = 256;
constexpr int K2_ = D_ + F_;

#define DI __device__ __forceinline__

DI float bf2f(u16 v) { return __uint_as_float((unsigned)v << 16); }
DI u16 f2bf(float f) { __hip_bfloat16 h = __float2bfloat16(f); return __bfloat16_as_ushort(h); }
DI float rbf(float f) { return bf2f(f2bf(f)); }
DI unsigned pack2(float a, float b) { return (unsigned)f2bf(a) | ((unsigned)f2bf(b) << 16); }

__global__ __launch_bounds__(256) void ln_kernel(const u16* __restrict__ x, u16* __restrict__ xn) {
  const int wave = threadIdx.x >> 6, lane = threadIdx.x & 63;
  const int row = blockIdx.x * 4 + wave;
  const u16* xr = x + (size_t)row * D_;
  bf16x8 v0 = *reinterpret_cast<const bf16x8*>(xr + lane * 8);
  bf16x8 v1 = *reinterpret_cast<const bf16x8*>(xr + 512 + lane * 8);
  float f[16];
#pragma unroll
  for (int j = 0; j < 8; ++j) { f[j] = bf2f((u16)v0[j]); f[8 + j] = bf2f((u16)v1[j]); }
  float s = 0.f;
#pragma unroll
  for (int j = 0; j < 16; ++j) s += f[j];
#pragma unroll
  for (int o = 32; o > 0; o >>= 1) s += __shfl_xor(s, o);
  const float mean = s * (1.0f / D_);
  float q = 0.f;
#pragma unroll
  for (int j = 0; j < 16; ++j) { float d = f[j] - mean; q += d * d; }
#pragma unroll
  for (int o = 32; o > 0; o >>= 1) q += __shfl_xor(q, o);
  const float var = q * (1.0f / D_);
  const float mb = rbf(mean);
  const float vb = rbf(var);
  const float sd = rbf(sqrtf(rbf(vb + 1e-5f)));
  u32x4 o0, o1;
#pragma unroll
  for (int j = 0; j < 4; ++j) {
    o0[j] = pack2(rbf(f[2 * j] - mb) / sd, rbf(f[2 * j + 1] - mb) / sd);
    o1[j] = pack2(rbf(f[8 + 2 * j] - mb) / sd, rbf(f[8 + 2 * j + 1] - mb) / sd);
  }
  u16* yr = xn + (size_t)row * D_;
  *reinterpret_cast<u32x4*>(yr + lane * 8) = o0;
  *reinterpret_cast<u32x4*>(yr + 512 + lane * 8) = o1;
}

DI int a_lds_byte(int r, int c) {
  int st = (r >> 4) * 2 + (c >> 5), rr = r & 15, cc = c & 31, ob = rr * 64 + cc * 2;
  return st * 1024 + (ob ^ (((ob >> 9) & 1) << 5));
}

#define WAIT_V(n) asm volatile("s_waitcnt vmcnt(" #n ")" ::: "memory")
#define WAIT_L(n) asm volatile("s_waitcnt lgkmcnt(" #n ")" ::: "memory")
#define BAR asm volatile("s_barrier" ::: "memory")
#define SCHED __builtin_amdgcn_sched_barrier(0)

typedef __attribute__((address_space(3))) const char* lds_cptr;
DI s16x4 tr_read(lds_cptr p) {
  return __builtin_amdgcn_ds_read_tr16_b64_v4i16((__attribute__((address_space(3))) s16x4*)(p));
}
DI bf16x8 lds_read16(lds_cptr p) { return *(const __attribute__((address_space(3))) bf16x8*)p; }
DI void glds16(const void* g, unsigned lds_base) {
  unsigned sv;
  asm volatile("s_mov_b32 %0, m0\n\ts_mov_b32 m0, %2\n\ts_nop 0\n\tglobal_load_lds_dwordx4 %1, off\n\ts_mov_b32 m0, %0"
               : "=&s"(sv) : "v"(g), "s"(lds_base) : "memory");
}
#define MFMA16(a, b, c) __builtin_amdgcn_mfma_f32_16x16x32_bf16((a), (b), (c), 0, 0, 0)

constexpr int HTB = 128 * 64 * 2;
constexpr int BIG_LDS = 8 * HTB;

template <bool SWAP>
DI void big_mainloop(const u16* __restrict__ Ag, const u16* __restrict__ Bh0, const u16* __restrict__ Bh1, int ldb,
                     char* shm, f32x4 (&acc)[2][2][4][2]) {
  int T = threadIdx.x; asm volatile("" : "+v"(T));
  const int wid = T >> 6, lane = T & 63, wr = wid >> 2, wc = wid & 3, fr = lane & 15, fq = lane >> 4;
  const unsigned lds0 = (unsigned)(uintptr_t)shm;
  const unsigned wbase = __builtin_amdgcn_readfirstlane(lds0 + (T >> 6) * 1024);
  const int sb = lane * 16, swz = sb ^ (((sb >> 9) & 1) << 5);
  const u16* a_src = Ag + (size_t)((T >> 7) * 16 + (swz >> 6)) * D_ + ((T >> 6) & 1) * 32 + ((swz & 63) >> 1);
  const int boff = ((T >> 7) * 8 + ((((T & 15) >> 1)) ^ (((T >> 7) & 1) * 4))) * ldb + ((T >> 4) & 7) * 16 + (T & 1) * 8;
  const u16* b_src0 = Bh0 + boff;
  const ptrdiff_t bdelta = Bh1 - Bh0;
#define SA_(b, h) (((b) * 2 + (h)) * HTB)
#define SB_(b, h) ((4 + (b) * 2 + (h)) * HTB)
#define STG_A(b, h, kt) do { const u16* _p = a_src + (size_t)(h) * 128 * D_ + (kt) * 64; \
    glds16(_p, wbase + SA_(b, h)); glds16(_p + (size_t)64 * D_, wbase + SA_(b, h) + 8192); } while (0)
#define STG_B(b, h, kt) do { const u16* _p = b_src0 + ((h) ? bdelta : 0) + (size_t)(kt) * 64 * ldb; \
    glds16(_p, wbase + SB_(b, h)); glds16(_p + (size_t)32 * ldb, wbase + SB_(b, h) + 8192); } while (0)
  const lds_cptr lbase = (lds_cptr)shm;
  const int q4 = fr >> 2, p4 = fr & 3;
  const lds_cptr bp0 = lbase + (fq * 8 + wc * 2) * 256 + ((q4 ^ ((fq & 1) * 4)) * 32) + p4 * 8;
  const lds_cptr bp1 = lbase + (fq * 8 + wc * 2) * 256 + (((4 + q4) ^ ((fq & 1) * 4)) * 32) + p4 * 8;
  const lds_cptr ap = lbase + a_lds_byte(wr * 64 + fr, fq * 8);
#define LDA_(dst, b, h) _Pragma("unroll") for (int m = 0; m < 4; ++m) _Pragma("unroll") for (int k = 0; k < 2; ++k) \
    dst[m][k] = lds_read16(ap + SA_(b, h) + m * 2048 + k * 1024)
#define LDB_(dst, b, h) _Pragma("unroll") for (int n = 0; n < 2; ++n) _Pragma("unroll") for (int k = 0; k < 2; ++k) { \
    s16x4 lo_ = tr_read(bp0 + SB_(b, h) + k * 8192 + n * 256); s16x4 hi_ = tr_read(bp1 + SB_(b, h) + k * 8192 + n * 256); \
    dst[n][k] = __builtin_shufflevector(lo_, hi_, 0, 1, 2, 3, 4, 5, 6, 7); }
#define MMA_(ai, bj, Af, Bf) do { __builtin_amdgcn_s_setprio(1); \
    _Pragma("unroll") for (int m = 0; m < 4; ++m) _Pragma("unroll") for (int n = 0; n < 2; ++n) _Pragma("unroll") for (int k = 0; k < 2; ++k) \
      acc[ai][bj][m][n] = SWAP ? MFMA16(Bf[n][k], Af[m][k], acc[ai][bj][m][n]) : MFMA16(Af[m][k], Bf[n][k], acc[ai][bj][m][n]); \
    __builtin_amdgcn_s_setprio(0); } while (0)

  bf16x8 At[4][2], B0[2][2], B1[2][2];
  constexpr int nt = D_ / 64;
  STG_B(0, 0, 0); STG_A(0, 0, 0); STG_B(0, 1, 0); STG_A(0, 1, 0);
  if (wr == 1) BAR;
  WAIT_V(4); BAR;
  STG_B(1, 0, 1); STG_A(1, 0, 1); STG_B(1, 1, 1);
  WAIT_V(6); BAR;
  for (int t = 0; t < nt - 2; t += 2) {
    LDB_(B0, 0, 0); SCHED; LDA_(At, 0, 0); STG_A(1, 1, t + 1);
    WAIT_L(8); BAR; WAIT_L(0); SCHED; MMA_(0, 0, At, B0); BAR; SCHED;
    LDB_(B1, 0, 1); STG_B(0, 0, t + 2);
    BAR; WAIT_L(0); SCHED; MMA_(0, 1, At, B1); BAR;
    LDA_(At, 0, 1); STG_A(0, 0, t + 2);
    BAR; WAIT_L(0); SCHED; MMA_(1, 0, At, B0); BAR; SCHED;
    STG_B(0, 1, t + 2);
    WAIT_V(6); BAR; MMA_(1, 1, At, B1); BAR;
    LDB_(B0, 1, 0); SCHED; LDA_(At, 1, 0); STG_A(0, 1, t + 2);
    WAIT_L(8); BAR; WAIT_L(0); SCHED; MMA_(0, 0, At, B0); BAR; SCHED;
    LDB_(B1, 1, 1); STG_B(1, 0, t + 3);
    BAR; WAIT_L(0); SCHED; MMA_(0, 1, At, B1); BAR;
    LDA_(At, 1, 1); STG_A(1, 0, t + 3);
    BAR; WAIT_L(0); SCHED; MMA_(1, 0, At, B0); BAR; SCHED;
    STG_B(1, 1, t + 3);
    WAIT_V(6); BAR; MMA_(1, 1, At, B1); BAR;
  }
  { LDB_(B0, 0, 0); WAIT_V(0); LDA_(At, 0, 0); STG_A(1, 1, nt - 1);
    BAR; WAIT_L(0); SCHED; MMA_(0, 0, At, B0); BAR;
    LDB_(B1, 0, 1); BAR; WAIT_L(0); SCHED; MMA_(0, 1, At, B1); BAR;
    LDA_(At, 0, 1); WAIT_V(4); BAR; WAIT_L(0); SCHED; MMA_(1, 0, At, B0); MMA_(1, 1, At, B1); BAR; }
  { LDB_(B0, 1, 0); LDA_(At, 1, 0); WAIT_V(2); BAR; WAIT_L(0); SCHED; MMA_(0, 0, At, B0); BAR;
    LDB_(B1, 1, 1); WAIT_V(0); BAR; WAIT_L(0); SCHED; MMA_(0, 1, At, B1); BAR;
    LDA_(At, 1, 1); BAR; WAIT_L(0); SCHED; MMA_(1, 0, At, B0); MMA_(1, 1, At, B1); BAR; }
  if (wr == 0) BAR;
#undef SA_
#undef SB_
#undef STG_A
#undef STG_B
#undef LDA_
#undef LDB_
#undef MMA_
}

__global__ __launch_bounds__(512, 2) void gemm1_big_kernel(
    const u16* __restrict__ xn, const u16* __restrict__ wqkv, const u16* __restrict__ wg, const u16* __restrict__ wu,
    u16* __restrict__ o_qk, u16* __restrict__ o_vt, u16* __restrict__ o_a2) {
  extern __shared__ __attribute__((aligned(16))) char shm[];
  const int b = blockIdx.x, xcd = b & 7, idx = b >> 3;
  const int mt = idx & 7, nt = xcd * 4 + (idx >> 3);
  const int brow = mt * 256;
  f32x4 acc[2][2][4][2];
#pragma unroll
  for (int a = 0; a < 2; ++a)
#pragma unroll
    for (int c = 0; c < 2; ++c)
#pragma unroll
      for (int m = 0; m < 4; ++m)
#pragma unroll
        for (int n = 0; n < 2; ++n) acc[a][c][m][n] = f32x4{0.f, 0.f, 0.f, 0.f};
  const u16* Ag = xn + (size_t)brow * D_;
  const bool is_v = (nt >= 8 && nt < 12);
  const bool is_gu = nt >= 12;
  const u16 *bh0, *bh1; int ldb;
  if (is_gu) { const int f0 = (nt - 12) * 128; bh0 = wg + f0; bh1 = wu + f0; ldb = F_; }
  else { bh0 = wqkv + nt * 256; bh1 = bh0 + 128; ldb = 3072; }
  if (is_v) big_mainloop<false>(Ag, bh0, bh1, ldb, shm, acc);
  else big_mainloop<true>(Ag, bh0, bh1, ldb, shm, acc);

  int T = threadIdx.x; asm volatile("" : "+v"(T));
  const int wid = T >> 6, lane = T & 63, wr = wid >> 2, wc = wid & 3, fr = lane & 15, fq = lane >> 4;
  if (is_gu) {
    const int f0 = (nt - 12) * 128;
#pragma unroll
    for (int ai = 0; ai < 2; ++ai)
#pragma unroll
      for (int m = 0; m < 4; ++m) {
        const int row = brow + ai * 128 + wr * 64 + m * 16 + fr;
#pragma unroll
        for (int n = 0; n < 2; ++n) {
          float hv[4];
#pragma unroll
          for (int j = 0; j < 4; ++j) {
            float g = rbf(acc[ai][0][m][n][j]), u = rbf(acc[ai][1][m][n][j]);
            hv[j] = (g > 0.f ? g : 0.f) * u;
          }
          u32x2 o; o[0] = pack2(hv[0], hv[1]); o[1] = pack2(hv[2], hv[3]);
          *reinterpret_cast<u32x2*>(o_a2 + (size_t)row * K2_ + D_ + f0 + wc * 32 + n * 16 + fq * 4) = o;
        }
      }
  } else if (!is_v) {
#pragma unroll
    for (int ai = 0; ai < 2; ++ai)
#pragma unroll
      for (int m = 0; m < 4; ++m) {
        const int row = brow + ai * 128 + wr * 64 + m * 16 + fr;
#pragma unroll
        for (int bj = 0; bj < 2; ++bj)
#pragma unroll
          for (int n = 0; n < 2; ++n) {
            const f32x4 v = acc[ai][bj][m][n];
            u32x2 o; o[0] = pack2(v[0], v[1]); o[1] = pack2(v[2], v[3]);
            *reinterpret_cast<u32x2*>(o_qk + (size_t)row * 2048 + nt * 256 + bj * 128 + wc * 32 + n * 16 + fq * 4) = o;
          }
      }
  } else {
#pragma unroll
    for (int ai = 0; ai < 2; ++ai)
#pragma unroll
      for (int m = 0; m < 4; ++m) {
        const int row = brow + ai * 128 + wr * 64 + m * 16 + fq * 4;
#pragma unroll
        for (int bj = 0; bj < 2; ++bj)
#pragma unroll
          for (int n = 0; n < 2; ++n) {
            const f32x4 v = acc[ai][bj][m][n];
            u32x2 o; o[0] = pack2(v[0], v[1]); o[1] = pack2(v[2], v[3]);
            const int col = (nt - 8) * 256 + bj * 128 + wc * 32 + n * 16 + fr;
            *reinterpret_cast<u32x2*>(o_vt + (size_t)col * S_ + row) = o;
          }
      }
  }
}

constexpr int G_STAGE = 32768;
constexpr int G_NS = 4;

template <int MODE>
__global__ __launch_bounds__(256, 1) void gemm128_kernel(
    const u16* __restrict__ A, const u16* __restrict__ B0g, const u16* __restrict__ B1g,
    u16* __restrict__ o_a2, float* __restrict__ o_part) {
  extern __shared__ __attribute__((aligned(16))) char shm[];
  const int T = threadIdx.x, wid = T >> 6, lane = T & 63, wr = wid >> 1, wc = wid & 1, fr = lane & 15, fq = lane >> 4;
  const int b = blockIdx.x, xcd = b & 7, idx = b >> 3;
  int mt, nt, kbeg, nkt, lda;
  if (MODE == 0) { mt = idx & 15; nt = xcd * 2 + (idx >> 4); kbeg = 0; nkt = D_ / 64; lda = D_; }
  else { mt = idx & 15; nt = xcd; kbeg = (idx >> 4) * (K2_ / 2); nkt = K2_ / 2 / 64; lda = K2_; }
  const int m0 = mt * 128;
  const unsigned lds0 = (unsigned)(uintptr_t)shm;
  const unsigned wbase = __builtin_amdgcn_readfirstlane(lds0 + wid * 1024);
  const int sb = lane * 16, swz = sb ^ (((sb >> 9) & 1) << 5);
  const u16* a_src = A + (size_t)(m0 + (T >> 7) * 16 + (swz >> 6)) * lda + kbeg + ((T >> 6) & 1) * 32 + ((swz & 63) >> 1);
  const int nb = (T >> 4) & 7, brow = (T >> 7) * 8 + (((T & 15) >> 1) ^ (((T >> 7) & 1) * 4)), hc = T & 1;
  const u16* b_src;
  int ldb;
  const int f0 = 2560 + nt * 64;
  if (MODE == 0) { b_src = ((nb & 1) ? B1g : B0g) + (size_t)brow * F_ + f0 + (nb >> 1) * 16 + hc * 8; ldb = F_; }
  else { b_src = B0g + (size_t)brow * D_ + nt * 128 + nb * 16 + hc * 8; ldb = D_; }
  const ptrdiff_t b_delta = (MODE == 1) ? (B1g - B0g) - (ptrdiff_t)D_ * D_ : 0;

#define STG(kt, slot) do { const u16* _a = a_src + (kt) * 64; \
    const int _kg = kbeg + (kt) * 64; \
    const u16* _b = b_src + (size_t)_kg * ldb + ((MODE == 1 && _kg >= D_) ? b_delta : 0); \
    const unsigned _d = wbase + (slot) * G_STAGE; \
    _Pragma("unroll") for (int _i = 0; _i < 4; ++_i) glds16(_a + (size_t)_i * 32 * lda, _d + _i * 4096); \
    _Pragma("unroll") for (int _i = 0; _i < 4; ++_i) glds16(_b + (size_t)_i * 16 * ldb, _d + 16384 + _i * 4096); } while (0)

  const lds_cptr lbase = (lds_cptr)shm;
  const int q4 = fr >> 2, p4 = fr & 3;
  const lds_cptr bp0 = lbase + 16384 + (fq * 8 + wc * 4) * 256 + ((q4 ^ ((fq & 1) * 4)) * 32) + p4 * 8;
  const lds_cptr bp1 = lbase + 16384 + (fq * 8 + wc * 4) * 256 + (((4 + q4) ^ ((fq & 1) * 4)) * 32) + p4 * 8;
  const lds_cptr ap = lbase + a_lds_byte(wr * 64 + fr, fq * 8);
#define RD(Af, Bf, so, ks) do { \
    _Pragma("unroll") for (int m = 0; m < 4; ++m) Af[m] = lds_read16(ap + (so) + m * 2048 + (ks) * 1024); \
    _Pragma("unroll") for (int n = 0; n < 4; ++n) { s16x4 lo_ = tr_read(bp0 + (so) + (ks) * 8192 + n * 256); s16x4 hi_ = tr_read(bp1 + (so) + (ks) * 8192 + n * 256); \
      Bf[n] = __builtin_shufflevector(lo_, hi_, 0, 1, 2, 3, 4, 5, 6, 7); } } while (0)
#define MM(Af, Bf) do { _Pragma("unroll") for (int m = 0; m < 4; ++m) _Pragma("unroll") for (int n = 0; n < 4; ++n) \
    acc[m][n] = MFMA16(Bf[n], Af[m], acc[m][n]); } while (0)

  f32x4 acc[4][4];
#pragma unroll
  for (int m = 0; m < 4; ++m)
#pragma unroll
    for (int n = 0; n < 4; ++n) acc[m][n] = f32x4{0.f, 0.f, 0.f, 0.f};
  bf16x8 AX[4], BX[4], AY[4], BY[4];

  STG(0, 0); STG(1, 1); STG(2, 2);
  WAIT_V(16); BAR;
  RD(AX, BX, 0, 0); RD(AY, BY, 0, 1);
  int so = 0;
  for (int kt = 0; kt < nkt; ++kt) {
    const int so_n = (so + G_STAGE) & (G_NS * G_STAGE - 1);
    MM(AX, BX);
    SCHED;
    if (kt + 1 < nkt) {
      if (kt + 2 < nkt) { WAIT_V(8); } else { WAIT_V(0); }
      BAR;
      if (kt + 3 < nkt) STG(kt + 3, (kt + 3) & (G_NS - 1));
      RD(AX, BX, so_n, 0);
    }
    SCHED;
    MM(AY, BY);
    SCHED;
    if (kt + 1 < nkt) RD(AY, BY, so_n, 1);
    so = so_n;
  }
#undef STG
#undef RD
#undef MM
  if (MODE == 0) {
#pragma unroll
    for (int m = 0; m < 4; ++m) {
      const int row = m0 + wr * 64 + m * 16 + fr;
#pragma unroll
      for (int n2 = 0; n2 < 2; ++n2) {
        float hv[4];
#pragma unroll
        for (int j = 0; j < 4; ++j) {
          float g = rbf(acc[m][2 * n2][j]), u = rbf(acc[m][2 * n2 + 1][j]);
          hv[j] = (g > 0.f ? g : 0.f) * u;
        }
        u32x2 o; o[0] = pack2(hv[0], hv[1]); o[1] = pack2(hv[2], hv[3]);
        *reinterpret_cast<u32x2*>(o_a2 + (size_t)row * K2_ + D_ + f0 + (wc * 2 + n2) * 16 + fq * 4) = o;
      }
    }
  } else {
    float* P = o_part + (size_t)(idx >> 4) * S_ * D_;
#pragma unroll
    for (int m = 0; m < 4; ++m) {
      const int row = m0 + wr * 64 + m * 16 + fr;
#pragma unroll
      for (int n = 0; n < 4; ++n)
        *reinterpret_cast<f32x4*>(P + (size_t)row * D_ + nt * 128 + wc * 64 + n * 16 + fq * 4) = acc[m][n];
    }
  }
}

__global__ __launch_bounds__(256) void attn_kernel(const u16* __restrict__ qk, const u16* __restrict__ vt, u16* __restrict__ a2) {
  const int wid = threadIdx.x >> 6, lane = threadIdx.x & 63;
  const int item = blockIdx.x * 4 + wid;
  const int head = item >> 6, qb = item & 63;
  const int q0 = qb * 32;
  const int c = lane & 31, h = lane >> 5;
  const u16* qp = qk + (size_t)(q0 + c) * 2048 + head * 32 + h * 8;
  const bf16x8 qf0 = *reinterpret_cast<const bf16x8*>(qp);
  const bf16x8 qf1 = *reinterpret_cast<const bf16x8*>(qp + 16);
  const int tfirst = (q0 >= WIN_) ? 0 : (WIN_ - q0) / 32;

  f32x16 sc[9];
  const float NEG = -1e30f;
#pragma unroll
  for (int t = 0; t < 9; ++t) {
    f32x16 z;
#pragma unroll
    for (int i = 0; i < 16; ++i) z[i] = NEG;
    if (t >= tfirst) {
      const int key0 = q0 - WIN_ + 32 * t;
      const u16* kp = qk + (size_t)(key0 + c) * 2048 + 1024 + head * 32 + h * 8;
      const bf16x8 kf0 = *reinterpret_cast<const bf16x8*>(kp);
      const bf16x8 kf1 = *reinterpret_cast<const bf16x8*>(kp + 16);
#pragma unroll
      for (int i = 0; i < 16; ++i) z[i] = 0.f;
      z = __builtin_amdgcn_mfma_f32_32x32x16_bf16(kf0, qf0, z, 0, 0, 0);
      z = __builtin_amdgcn_mfma_f32_32x32x16_bf16(kf1, qf1, z, 0, 0, 0);
      if (t == 0) {
#pragma unroll
        for (int i = 0; i < 16; ++i) { const int kl = (i & 3) + 8 * (i >> 2) + 4 * h; if (kl <= c) z[i] = NEG; }
      }
      if (t == 8) {
#pragma unroll
        for (int i = 0; i < 16; ++i) { const int kl = (i & 3) + 8 * (i >> 2) + 4 * h; if (kl > c) z[i] = NEG; }
      }
    }
    sc[t] = z;
  }
  float mx = NEG;
#pragma unroll
  for (int t = 0; t < 9; ++t)
#pragma unroll
    for (int i = 0; i < 16; ++i) mx = fmaxf(mx, sc[t][i]);
  mx = fmaxf(mx, __shfl_xor(mx, 32));
  const float cs = 0.17677669529663687f * 1.4426950408889634f;
  float sum = 0.f;
#pragma unroll
  for (int t = 0; t < 9; ++t)
#pragma unroll
    for (int i = 0; i < 16; ++i) { float p = exp2f((sc[t][i] - mx) * cs); sc[t][i] = p; sum += p; }
  sum += __shfl_xor(sum, 32);

  f32x16 o;
#pragma unroll
  for (int i = 0; i < 16; ++i) o[i] = 0.f;
  const u16* vbase = vt + (size_t)(head * 32 + c) * S_;
#pragma unroll
  for (int t = 0; t < 9; ++t) {
    if (t >= tfirst) {
      const int key0 = q0 - WIN_ + 32 * t;
#pragma unroll
      for (int s = 0; s < 2; ++s) {
        const u16* vp = vbase + key0 + 16 * s + 4 * h;
        const s16x4 lo = *reinterpret_cast<const s16x4*>(vp);
        const s16x4 hi = *reinterpret_cast<const s16x4*>(vp + 8);
        const bf16x8 vf = __builtin_shufflevector(lo, hi, 0, 1, 2, 3, 4, 5, 6, 7);
        u32x4 pk;
#pragma unroll
        for (int j = 0; j < 4; ++j) pk[j] = pack2(sc[t][8 * s + 2 * j], sc[t][8 * s + 2 * j + 1]);
        const bf16x8 pf = __builtin_bit_cast(bf16x8, pk);
        o = __builtin_amdgcn_mfma_f32_32x32x16_bf16(vf, pf, o, 0, 0, 0);
      }
    }
  }
  const float inv = 1.0f / sum;
  u16* op = a2 + (size_t)(q0 + c) * K2_ + head * 32 + 4 * h;
#pragma unroll
  for (int g = 0; g < 4; ++g) {
    u32x2 w; w[0] = pack2(o[4 * g] * inv, o[4 * g + 1] * inv); w[1] = pack2(o[4 * g + 2] * inv, o[4 * g + 3] * inv);
    *reinterpret_cast<u32x2*>(op + 8 * g) = w;
  }
}

__global__ __launch_bounds__(256) void reduce_kernel(const u16* __restrict__ x, const float* __restrict__ part, u16* __restrict__ out) {
  const size_t i = ((size_t)blockIdx.x * 256 + threadIdx.x) * 8;
  const bf16x8 xv = *reinterpret_cast<const bf16x8*>(x + i);
  const f32x4 a0 = *reinterpret_cast<const f32x4*>(part + i), a1 = *reinterpret_cast<const f32x4*>(part + i + 4);
  const f32x4 b0 = *reinterpret_cast<const f32x4*>(part + (size_t)S_ * D_ + i), b1 = *reinterpret_cast<const f32x4*>(part + (size_t)S_ * D_ + i + 4);
  u32x4 o;
#pragma unroll
  for (int j = 0; j < 2; ++j) {
    o[j] = pack2(bf2f((u16)xv[2 * j]) + a0[2 * j] + b0[2 * j], bf2f((u16)xv[2 * j + 1]) + a0[2 * j + 1] + b0[2 * j + 1]);
    o[2 + j] = pack2(bf2f((u16)xv[4 + 2 * j]) + a1[2 * j] + b1[2 * j], bf2f((u16)xv[4 + 2 * j + 1]) + a1[2 * j + 1] + b1[2 * j + 1]);
  }
  *reinterpret_cast<u32x4*>(out + i) = o;
}

extern "C" void kernel_launch(void* const* d_in, const int* in_sizes, int n_in, void* d_out, int out_size, void* d_ws,
                              size_t ws_size, hipStream_t stream) {
  const u16* x = (const u16*)d_in[0];
  const u16* wqkv = (const u16*)d_in[1];
  const u16* wo = (const u16*)d_in[2];
  const u16* wg = (const u16*)d_in[4];
  const u16* wu = (const u16*)d_in[5];
  const u16* wd = (const u16*)d_in[6];
  char* ws = (char*)d_ws;
  u16* xn = (u16*)(ws);
  u16* qk = (u16*)(ws + (4u << 20));
  u16* vt = (u16*)(ws + (12u << 20));
  u16* a2 = (u16*)(ws + (16u << 20));
  float* part = (float*)(ws + (36u << 20));

  (void)hipFuncSetAttribute((const void*)gemm1_big_kernel, hipFuncAttributeMaxDynamicSharedMemorySize, BIG_LDS);
  (void)hipFuncSetAttribute((const void*)gemm128_kernel<0>, hipFuncAttributeMaxDynamicSharedMemorySize, G_NS * G_STAGE);
  (void)hipFuncSetAttribute((const void*)gemm128_kernel<1>, hipFuncAttributeMaxDynamicSharedMemorySize, G_NS * G_STAGE);

  ln_kernel<<<S_ / 4, 256, 0, stream>>>(x, xn);
  gemm1_big_kernel<<<256, 512, BIG_LDS, stream>>>(xn, wqkv, wg, wu, qk, vt, a2);
  gemm128_kernel<0><<<256, 256, G_NS * G_STAGE, stream>>>(xn, wg, wu, a2, nullptr);
  attn_kernel<<<2048 / 4, 256, 0, stream>>>(qk, vt, a2);
  gemm128_kernel<1><<<256, 256, G_NS * G_STAGE, stream>>>(a2, wo, wd, nullptr, part);
  reduce_kernel<<<S_ * D_ / 8 / 256, 256, 0, stream>>>(x, part, (u16*)d_out);
}
```

```cpp
#include <hip/hip_runtime.h>
#include <hip/hip_bf16.h>
#include <stdint.h>

typedef __attribute__((ext_vector_type(8))) short bf16x8;
typedef __attribute__((ext_vector_type(4))) short s16x4;
typedef __attribute__((ext_vector_type(4))) float f32x4;
typedef __attribute__((ext_vector_type(16))) float f32x16;
typedef __attribute__((ext_vector_type(4))) unsigned u32x4;
typedef __attribute__((ext_vector_type(2))) unsigned u32x2;
typedef unsigned short u16;

constexpr int S_ = 2048, D_ = 1024, F_ = 3584, H_ = 32, DH_ = 32, WIN_ = 256;
constexpr int K2_ = D_ + F_;

#define DI __device__ __forceinline__

DI float bf2f(u16 v) { return __uint_as_float((unsigned)v << 16); }
DI u16 f2bf(float f) { __hip_bfloat16 h = __float2bfloat16(f); return __bfloat16_as_ushort(h); }
DI float rbf(float f) { return bf2f(f2bf(f)); }
DI unsigned pack2(float a, float b) { return (unsigned)f2bf(a) | ((unsigned)f2bf(b) << 16); }

__global__ __launch_bounds__(256) void ln_kernel(const u16* __restrict__ x, u16* __restrict__ xn) {
  const int wave = threadIdx.x >> 6, lane = threadIdx.x & 63;
  const int row = blockIdx.x * 4 + wave;
  const u16* xr = x + (size_t)row * D_;
  bf16x8 v0 = *reinterpret_cast<const bf16x8*>(xr + lane * 8);
  bf16x8 v1 = *reinterpret_cast<const bf16x8*>(xr + 512 + lane * 8);
  float f[16];
#pragma unroll
  for (int j = 0; j < 8; ++j) { f[j] = bf2f((u16)v0[j]); f[8 + j] = bf2f((u16)v1[j]); }
  float s = 0.f;
#pragma unroll
  for (int j = 0; j < 16; ++j) s += f[j];
#pragma unroll
  for (int o = 32; o > 0; o >>= 1) s += __shfl_xor(s, o);
  const float mean = s * (1.0f / D_);
  float q = 0.f;
#pragma unroll
  for (int j = 0; j < 16; ++j) { float d = f[j] - mean; q += d * d; }
#pragma unroll
  for (int o = 32; o > 0; o >>= 1) q += __shfl_xor(q, o);
  const float var = q * (1.0f / D_);
  const float mb = rbf(mean);
  const float vb = rbf(var);
  const float sd = rbf(sqrtf(rbf(vb + 1e-5f)));
  u32x4 o0, o1;
#pragma unroll
  for (int j = 0; j < 4; ++j) {
    o0[j] = pack2(rbf(f[2 * j] - mb) / sd, rbf(f[2 * j + 1] - mb) / sd);
    o1[j] = pack2(rbf(f[8 + 2 * j] - mb) / sd, rbf(f[8 + 2 * j + 1] - mb) / sd);
  }
  u16* yr = xn + (size_t)row * D_;
  *reinterpret_cast<u32x4*>(yr + lane * 8) = o0;
  *reinterpret_cast<u32x4*>(yr + 512 + lane * 8) = o1;
}

DI int a_lds_byte(int r, int c) {
  int st = (r >> 4) * 2 + (c >> 5), rr = r & 15, cc = c & 31, ob = rr * 64 + cc * 2;
  return st * 1024 + (ob ^ (((ob >> 9) & 1) << 5));
}

#define WAIT_V(n) asm volatile("s_waitcnt vmcnt(" #n ")" ::: "memory")
#define WAIT_L(n) asm volatile("s_waitcnt lgkmcnt(" #n ")" ::: "memory")
#define BAR asm volatile("s_barrier" ::: "memory")
#define SCHED __builtin_amdgcn_sched_barrier(0)

typedef __attribute__((address_space(3))) const char* lds_cptr;
DI s16x4 tr_read(lds_cptr p) {
  return __builtin_amdgcn_ds_read_tr16_b64_v4i16((__attribute__((address_space(3))) s16x4*)(p));
}
DI bf16x8 lds_read16(lds_cptr p) { return *(const __attribute__((address_space(3))) bf16x8*)p; }
DI void glds16(const void* g, unsigned lds_base) {
  unsigned sv;
  asm volatile("s_mov_b32 %0, m0\n\ts_mov_b32 m0, %2\n\ts_nop 0\n\tglobal_load_lds_dwordx4 %1, off\n\ts_mov_b32 m0, %0"
               : "=&s"(sv) : "v"(g), "s"(lds_base) : "memory");
}
#define MFMA16(a, b, c) __builtin_amdgcn_mfma_f32_16x16x32_bf16((a), (b), (c), 0, 0, 0)

constexpr int HTB = 128 * 64 * 2;
constexpr int BIG_LDS = 8 * HTB;

template <bool SWAP, bool HALFM>
DI void big_mainloop(const u16* __restrict__ Ag, const u16* __restrict__ Bh0, const u16* __restrict__ Bh1, int ldb,
                     char* shm, f32x4 (&acc)[2][2][4][2]) {
  int T = threadIdx.x; asm volatile("" : "+v"(T));
  const int wid = T >> 6, lane = T & 63, wr = wid >> 2, wc = wid & 3, fr = lane & 15, fq = lane >> 4;
  const unsigned lds0 = (unsigned)(uintptr_t)shm;
  const unsigned wbase = __builtin_amdgcn_readfirstlane(lds0 + (T >> 6) * 1024);
  const int sb = lane * 16, swz = sb ^ (((sb >> 9) & 1) << 5);
  const u16* a_src = Ag + (size_t)((T >> 7) * 16 + (swz >> 6)) * D_ + ((T >> 6) & 1) * 32 + ((swz & 63) >> 1);
  const int boff = ((T >> 7) * 8 + ((((T & 15) >> 1)) ^ (((T >> 7) & 1) * 4))) * ldb + ((T >> 4) & 7) * 16 + (T & 1) * 8;
  const u16* b_src0 = Bh0 + boff;
  const ptrdiff_t bdelta = Bh1 - Bh0;
#define SA_(b, h) (((b) * 2 + (h)) * HTB)
#define SB_(b, h) ((4 + (b) * 2 + (h)) * HTB)
#define STG_A(b, h, kt) do { if (HALFM && (h) == 1) break; const u16* _p = a_src + (size_t)(h) * 128 * D_ + (kt) * 64; \
    glds16(_p, wbase + SA_(b, h)); glds16(_p + (size_t)64 * D_, wbase + SA_(b, h) + 8192); } while (0)
#define STG_B(b, h, kt) do { const u16* _p = b_src0 + ((h) ? bdelta : 0) + (size_t)(kt) * 64 * ldb; \
    glds16(_p, wbase + SB_(b, h)); glds16(_p + (size_t)32 * ldb, wbase + SB_(b, h) + 8192); } while (0)
  const lds_cptr lbase = (lds_cptr)shm;
  const int q4 = fr >> 2, p4 = fr & 3;
  const lds_cptr bp0 = lbase + (fq * 8 + wc * 2) * 256 + ((q4 ^ ((fq & 1) * 4)) * 32) + p4 * 8;
  const lds_cptr bp1 = lbase + (fq * 8 + wc * 2) * 256 + (((4 + q4) ^ ((fq & 1) * 4)) * 32) + p4 * 8;
  const lds_cptr ap = lbase + a_lds_byte(wr * 64 + fr, fq * 8);
#define LDA_(dst, b, h) if (!(HALFM && (h) == 1)) _Pragma("unroll") for (int m = 0; m < 4; ++m) _Pragma("unroll") for (int k = 0; k < 2; ++k) \
    dst[m][k] = lds_read16(ap + SA_(b, h) + m * 2048 + k * 1024)
#define LDB_(dst, b, h) _Pragma("unroll") for (int n = 0; n < 2; ++n) _Pragma("unroll") for (int k = 0; k < 2; ++k) { \
    s16x4 lo_ = tr_read(bp0 + SB_(b, h) + k * 8192 + n * 256); s16x4 hi_ = tr_read(bp1 + SB_(b, h) + k * 8192 + n * 256); \
    dst[n][k] = __builtin_shufflevector(lo_, hi_, 0, 1, 2, 3, 4, 5, 6, 7); }
#define MMA_(ai, bj, Af, Bf) do { if (HALFM && (ai) == 1) break; __builtin_amdgcn_s_setprio(1); \
    _Pragma("unroll") for (int m = 0; m < 4; ++m) _Pragma("unroll") for (int n = 0; n < 2; ++n) _Pragma("unroll") for (int k = 0; k < 2; ++k) \
      acc[ai][bj][m][n] = SWAP ? MFMA16(Bf[n][k], Af[m][k], acc[ai][bj][m][n]) : MFMA16(Af[m][k], Bf[n][k], acc[ai][bj][m][n]); \
    __builtin_amdgcn_s_setprio(0); } while (0)

  bf16x8 At[4][2], B0[2][2], B1[2][2];
  constexpr int nt = D_ / 64;
  STG_B(0, 0, 0); STG_A(0, 0, 0); STG_B(0, 1, 0); STG_A(0, 1, 0);
  if (wr == 1) BAR;
  if (HALFM) { WAIT_V(2); } else { WAIT_V(4); }
  BAR;
  STG_B(1, 0, 1); STG_A(1, 0, 1); STG_B(1, 1, 1);
  WAIT_V(6); BAR;
  for (int t = 0; t < nt - 2; t += 2) {
    LDB_(B0, 0, 0); SCHED; LDA_(At, 0, 0); STG_A(1, 1, t + 1);
    WAIT_L(8); BAR; WAIT_L(0); SCHED; MMA_(0, 0, At, B0); BAR; SCHED;
    LDB_(B1, 0, 1); STG_B(0, 0, t + 2);
    BAR; WAIT_L(0); SCHED; MMA_(0, 1, At, B1); BAR;
    LDA_(At, 0, 1); STG_A(0, 0, t + 2);
    BAR; WAIT_L(0); SCHED; MMA_(1, 0, At, B0); BAR; SCHED;
    STG_B(0, 1, t + 2);
    WAIT_V(6); BAR; MMA_(1, 1, At, B1); BAR;
    LDB_(B0, 1, 0); SCHED; LDA_(At, 1, 0); STG_A(0, 1, t + 2);
    WAIT_L(8); BAR; WAIT_L(0); SCHED; MMA_(0, 0, At, B0); BAR; SCHED;
    LDB_(B1, 1, 1); STG_B(1, 0, t + 3);
    BAR; WAIT_L(0); SCHED; MMA_(0, 1, At, B1); BAR;
    LDA_(At, 1, 1); STG_A(1, 0, t + 3);
    BAR; WAIT_L(0); SCHED; MMA_(1, 0, At, B0); BAR; SCHED;
    STG_B(1, 1, t + 3);
    WAIT_V(6); BAR; MMA_(1, 1, At, B1); BAR;
  }
  { LDB_(B0, 0, 0); WAIT_V(0); LDA_(At, 0, 0); STG_A(1, 1, nt - 1);
    BAR; WAIT_L(0); SCHED; MMA_(0, 0, At, B0); BAR;
    LDB_(B1, 0, 1); BAR; WAIT_L(0); SCHED; MMA_(0, 1, At, B1); BAR;
    LDA_(At, 0, 1); WAIT_V(4); BAR; WAIT_L(0); SCHED; MMA_(1, 0, At, B0); MMA_(1, 1, At, B1); BAR; }
  { LDB_(B0, 1, 0); LDA_(At, 1, 0); WAIT_V(2); BAR; WAIT_L(0); SCHED; MMA_(0, 0, At, B0); BAR;
    LDB_(B1, 1, 1); WAIT_V(0); BAR; WAIT_L(0); SCHED; MMA_(0, 1, At, B1); BAR;
    LDA_(At, 1, 1); BAR; WAIT_L(0); SCHED; MMA_(1, 0, At, B0); MMA_(1, 1, At, B1); BAR; }
  if (wr == 0) BAR;
#undef SA_
#undef SB_
#undef STG_A
#undef STG_B
#undef LDA_
#undef LDB_
#undef MMA_
}

__global__ __launch_bounds__(512, 2) void gemm1_big_kernel(
    const u16* __restrict__ xn, const u16* __restrict__ wqkv, const u16* __restrict__ wg, const u16* __restrict__ wu,
    u16* __restrict__ o_qk, u16* __restrict__ o_vt, u16* __restrict__ o_a2) {
  extern __shared__ __attribute__((aligned(16))) char shm[];
  const int b = blockIdx.x, xcd = b & 7, idx = b >> 3;
  const int mt = idx & 7, nt = xcd * 4 + (idx >> 3);
  const int brow = mt * 256;
  f32x4 acc[2][2][4][2];
#pragma unroll
  for (int a = 0; a < 2; ++a)
#pragma unroll
    for (int c = 0; c < 2; ++c)
#pragma unroll
      for (int m = 0; m < 4; ++m)
#pragma unroll
        for (int n = 0; n < 2; ++n) acc[a][c][m][n] = f32x4{0.f, 0.f, 0.f, 0.f};
  const u16* Ag = xn + (size_t)brow * D_;
  const bool is_v = (nt >= 8 && nt < 12);
  const bool is_gu = nt >= 12;
  const u16 *bh0, *bh1; int ldb;
  if (is_gu) { const int f0 = (nt - 12) * 128; bh0 = wg + f0; bh1 = wu + f0; ldb = F_; }
  else { bh0 = wqkv + nt * 256; bh1 = bh0 + 128; ldb = 3072; }
  if (is_v) big_mainloop<false, false>(Ag, bh0, bh1, ldb, shm, acc);
  else big_mainloop<true, false>(Ag, bh0, bh1, ldb, shm, acc);

  int T = threadIdx.x; asm volatile("" : "+v"(T));
  const int wid = T >> 6, lane = T & 63, wr = wid >> 2, wc = wid & 3, fr = lane & 15, fq = lane >> 4;
  if (is_gu) {
    const int f0 = (nt - 12) * 128;
#pragma unroll
    for (int ai = 0; ai < 2; ++ai)
#pragma unroll
      for (int m = 0; m < 4; ++m) {
        const int row = brow + ai * 128 + wr * 64 + m * 16 + fr;
#pragma unroll
        for (int n = 0; n < 2; ++n) {
          float hv[4];
#pragma unroll
          for (int j = 0; j < 4; ++j) {
            float g = rbf(acc[ai][0][m][n][j]), u = rbf(acc[ai][1][m][n][j]);
            hv[j] = (g > 0.f ? g : 0.f) * u;
          }
          u32x2 o; o[0] = pack2(hv[0], hv[1]); o[1] = pack2(hv[2], hv[3]);
          *reinterpret_cast<u32x2*>(o_a2 + (size_t)row * K2_ + D_ + f0 + wc * 32 + n * 16 + fq * 4) = o;
        }
      }
  } else if (!is_v) {
#pragma unroll
    for (int ai = 0; ai < 2; ++ai)
#pragma unroll
      for (int m = 0; m < 4; ++m) {
        const int row = brow + ai * 128 + wr * 64 + m * 16 + fr;
#pragma unroll
        for (int bj = 0; bj < 2; ++bj)
#pragma unroll
          for (int n = 0; n < 2; ++n) {
            const f32x4 v = acc[ai][bj][m][n];
            u32x2 o; o[0] = pack2(v[0], v[1]); o[1] = pack2(v[2], v[3]);
            const int hd = nt * 8 + bj * 4 + wc;
            *reinterpret_cast<u32x2*>(o_qk + ((size_t)hd * S_ + row) * 32 + n * 16 + fq * 4) = o;
          }
      }
  } else {
#pragma unroll
    for (int ai = 0; ai < 2; ++ai)
#pragma unroll
      for (int m = 0; m < 4; ++m) {
        const int row = brow + ai * 128 + wr * 64 + m * 16 + fq * 4;
#pragma unroll
        for (int bj = 0; bj < 2; ++bj)
#pragma unroll
          for (int n = 0; n < 2; ++n) {
            const f32x4 v = acc[ai][bj][m][n];
            u32x2 o; o[0] = pack2(v[0], v[1]); o[1] = pack2(v[2], v[3]);
            const int col = (nt - 8) * 256 + bj * 128 + wc * 32 + n * 16 + fr;
            *reinterpret_cast<u32x2*>(o_vt + (size_t)col * S_ + row) = o;
          }
      }
  }
}

constexpr int G_STAGE = 32768;
constexpr int G_NS = 4;

template <int MODE>
__global__ __launch_bounds__(256, 2) void gemm128_kernel(
    const u16* __restrict__ A, const u16* __restrict__ B0g, const u16* __restrict__ B1g,
    u16* __restrict__ o_a2, float* __restrict__ o_part) {
  extern __shared__ __attribute__((aligned(16))) char shm[];
  const int T = threadIdx.x, wid = T >> 6, lane = T & 63, wr = wid >> 1, wc = wid & 1, fr = lane & 15, fq = lane >> 4;
  const int b = blockIdx.x, xcd = b & 7, idx = b >> 3;
  int mt, nt, kbeg, nkt, lda;
  if (MODE == 0) { mt = idx & 15; nt = xcd * 2 + (idx >> 4); kbeg = 0; nkt = D_ / 64; lda = D_; }
  else { mt = xcd * 2 + (idx & 1); nt = (idx >> 1) & 7; kbeg = (idx >> 4) * (K2_ / 2); nkt = K2_ / 2 / 64; lda = K2_; }
  const int m0 = mt * 128;
  const unsigned lds0 = (unsigned)(uintptr_t)shm;
  const unsigned wbase = __builtin_amdgcn_readfirstlane(lds0 + wid * 1024);
  const int sb = lane * 16, swz = sb ^ (((sb >> 9) & 1) << 5);
  const u16* a_src = A + (size_t)(m0 + (T >> 7) * 16 + (swz >> 6)) * lda + kbeg + ((T >> 6) & 1) * 32 + ((swz & 63) >> 1);
  const int nb = (T >> 4) & 7, brow = (T >> 7) * 8 + (((T & 15) >> 1) ^ (((T >> 7) & 1) * 4)), hc = T & 1;
  const u16* b_src;
  int ldb;
  const int f0 = 2560 + nt * 64;
  if (MODE == 0) { b_src = ((nb & 1) ? B1g : B0g) + (size_t)brow * F_ + f0 + (nb >> 1) * 16 + hc * 8; ldb = F_; }
  else { b_src = B0g + (size_t)brow * D_ + nt * 128 + nb * 16 + hc * 8; ldb = D_; }
  const ptrdiff_t b_delta = (MODE == 1) ? (B1g - B0g) - (ptrdiff_t)D_ * D_ : 0;

#define STG(kt, slot) do { const u16* _a = a_src + (kt) * 64; \
    const int _kg = kbeg + (kt) * 64; \
    const u16* _b = b_src + (size_t)_kg * ldb + ((MODE == 1 && _kg >= D_) ? b_delta : 0); \
    const unsigned _d = wbase + (slot) * G_STAGE; \
    _Pragma("unroll") for (int _i = 0; _i < 4; ++_i) glds16(_a + (size_t)_i * 32 * lda, _d + _i * 4096); \
    _Pragma("unroll") for (int _i = 0; _i < 4; ++_i) glds16(_b + (size_t)_i * 16 * ldb, _d + 16384 + _i * 4096); } while (0)

  const lds_cptr lbase = (lds_cptr)shm;
  const int q4 = fr >> 2, p4 = fr & 3;
  const lds_cptr bp0 = lbase + 16384 + (fq * 8 + wc * 4) * 256 + ((q4 ^ ((fq & 1) * 4)) * 32) + p4 * 8;
  const lds_cptr bp1 = lbase + 16384 + (fq * 8 + wc * 4) * 256 + (((4 + q4) ^ ((fq & 1) * 4)) * 32) + p4 * 8;
  const lds_cptr ap = lbase + a_lds_byte(wr * 64 + fr, fq * 8);
#define RD(Af, Bf, so, ks) do { \
    _Pragma("unroll") for (int m = 0; m < 4; ++m) Af[m] = lds_read16(ap + (so) + m * 2048 + (ks) * 1024); \
    _Pragma("unroll") for (int n = 0; n < 4; ++n) { s16x4 lo_ = tr_read(bp0 + (so) + (ks) * 8192 + n * 256); s16x4 hi_ = tr_read(bp1 + (so) + (ks) * 8192 + n * 256); \
      Bf[n] = __builtin_shufflevector(lo_, hi_, 0, 1, 2, 3, 4, 5, 6, 7); } } while (0)
#define MM(Af, Bf) do { _Pragma("unroll") for (int m = 0; m < 4; ++m) _Pragma("unroll") for (int n = 0; n < 4; ++n) \
    acc[m][n] = MFMA16(Bf[n], Af[m], acc[m][n]); } while (0)

  f32x4 acc[4][4];
#pragma unroll
  for (int m = 0; m < 4; ++m)
#pragma unroll
    for (int n = 0; n < 4; ++n) acc[m][n] = f32x4{0.f, 0.f, 0.f, 0.f};
  bf16x8 AX[4], BX[4], AY[4], BY[4];

  STG(0, 0); STG(1, 1); STG(2, 2);
  WAIT_V(16); BAR;
  RD(AX, BX, 0, 0); RD(AY, BY, 0, 1);
  int so = 0;
  for (int kt = 0; kt < nkt; ++kt) {
    const int so_n = (so + G_STAGE) & (G_NS * G_STAGE - 1);
    MM(AX, BX);
    SCHED;
    if (kt + 1 < nkt) {
      if (kt + 2 < nkt) { WAIT_V(8); } else { WAIT_V(0); }
      BAR;
      if (kt + 3 < nkt) STG(kt + 3, (kt + 3) & (G_NS - 1));
      RD(AX, BX, so_n, 0);
    }
    SCHED;
    MM(AY, BY);
    SCHED;
    if (kt + 1 < nkt) RD(AY, BY, so_n, 1);
    so = so_n;
  }
#undef STG
#undef RD
#undef MM
  if (MODE == 0) {
#pragma unroll
    for (int m = 0; m < 4; ++m) {
      const int row = m0 + wr * 64 + m * 16 + fr;
#pragma unroll
      for (int n2 = 0; n2 < 2; ++n2) {
        float hv[4];
#pragma unroll
        for (int j = 0; j < 4; ++j) {
          float g = rbf(acc[m][2 * n2][j]), u = rbf(acc[m][2 * n2 + 1][j]);
          hv[j] = (g > 0.f ? g : 0.f) * u;
        }
        u32x2 o; o[0] = pack2(hv[0], hv[1]); o[1] = pack2(hv[2], hv[3]);
        *reinterpret_cast<u32x2*>(o_a2 + (size_t)row * K2_ + D_ + f0 + (wc * 2 + n2) * 16 + fq * 4) = o;
      }
    }
  } else {
    float* P = o_part + (size_t)(idx >> 4) * S_ * D_;
#pragma unroll
    for (int m = 0; m < 4; ++m) {
      const int row = m0 + wr * 64 + m * 16 + fr;
#pragma unroll
      for (int n = 0; n < 4; ++n)
        *reinterpret_cast<f32x4*>(P + (size_t)row * D_ + nt * 128 + wc * 64 + n * 16 + fq * 4) = acc[m][n];
    }
  }
}

constexpr int AK_ROW = 80, AV_ROW = 1032;
DI void attn_body(int item, const u16* __restrict__ qk, const u16* __restrict__ vt, u16* __restrict__ a2, char* shm) {
  int T = threadIdx.x; asm volatile("" : "+v"(T));
  const int wid = T >> 6, lane = T & 63;
  const int head = item >> 3, qblk = item & 7;
  const int qb0 = qblk * 256, kbase = qb0 - WIN_;
  char* sK = shm;
  char* sV = shm + 512 * AK_ROW;
  {
    const u16* kg = qk + ((size_t)(32 + head) * S_ + kbase) * 32;
    bf16x8 tk[4], tv[4];
#pragma unroll
    for (int i = 0; i < 4; ++i) {
      const int cidx = i * 512 + T, r = cidx >> 2;
      tk[i] = (kbase + r >= 0) ? *reinterpret_cast<const bf16x8*>(kg + cidx * 8) : bf16x8{0, 0, 0, 0, 0, 0, 0, 0};
    }
#pragma unroll
    for (int i = 0; i < 4; ++i) {
      const int cidx = i * 512 + T, d = cidx >> 6, ch = cidx & 63;
      tv[i] = (kbase + ch * 8 >= 0) ? *reinterpret_cast<const bf16x8*>(vt + (size_t)(head * 32 + d) * S_ + kbase + ch * 8) : bf16x8{0, 0, 0, 0, 0, 0, 0, 0};
    }
#pragma unroll
    for (int i = 0; i < 4; ++i) { const int cidx = i * 512 + T; *reinterpret_cast<bf16x8*>(sK + (cidx >> 2) * AK_ROW + (cidx & 3) * 16) = tk[i]; }
#pragma unroll
    for (int i = 0; i < 4; ++i) { const int cidx = i * 512 + T, d = cidx >> 6, ch = cidx & 63; *reinterpret_cast<bf16x8*>(sV + d * AV_ROW + ch * 16) = tv[i]; }
  }
  const int q0 = qb0 + wid * 32;
  const int c = lane & 31, h = lane >> 5;
  const u16* qp = qk + ((size_t)head * S_ + q0 + c) * 32 + h * 8;
  const bf16x8 qf0 = *reinterpret_cast<const bf16x8*>(qp);
  const bf16x8 qf1 = *reinterpret_cast<const bf16x8*>(qp + 16);
  const int tfirst = (q0 >= WIN_) ? 0 : (WIN_ - q0) / 32;
  __syncthreads();

  f32x16 sc[9];
  const float NEG = -1e30f;
  const char* kr = sK + (wid * 32 + c) * AK_ROW + h * 16;
#pragma unroll
  for (int t = 0; t < 9; ++t) {
    f32x16 z;
    if (t >= tfirst) {
      const bf16x8 kf0 = *reinterpret_cast<const bf16x8*>(kr + t * 32 * AK_ROW);
      const bf16x8 kf1 = *reinterpret_cast<const bf16x8*>(kr + t * 32 * AK_ROW + 32);
#pragma unroll
      for (int i = 0; i < 16; ++i) z[i] = 0.f;
      z = __builtin_amdgcn_mfma_f32_32x32x16_bf16(kf0, qf0, z, 0, 0, 0);
      z = __builtin_amdgcn_mfma_f32_32x32x16_bf16(kf1, qf1, z, 0, 0, 0);
      if (t == 0) {
#pragma unroll
        for (int i = 0; i < 16; ++i) { const int kl = (i & 3) + 8 * (i >> 2) + 4 * h; if (kl <= c) z[i] = NEG; }
      }
      if (t == 8) {
#pragma unroll
        for (int i = 0; i < 16; ++i) { const int kl = (i & 3) + 8 * (i >> 2) + 4 * h; if (kl > c) z[i] = NEG; }
      }
    } else {
#pragma unroll
      for (int i = 0; i < 16; ++i) z[i] = NEG;
    }
    sc[t] = z;
  }
  float mx = NEG;
#pragma unroll
  for (int t = 0; t < 9; ++t)
#pragma unroll
    for (int i = 0; i < 16; i += 2) mx = __builtin_fmaxf(__builtin_fmaxf(mx, sc[t][i]), sc[t][i + 1]);
  mx = fmaxf(mx, __shfl_xor(mx, 32));
  const float cs = 0.17677669529663687f * 1.4426950408889634f;
  float sum = 0.f;
  const float nmc = -mx * cs;
#pragma unroll
  for (int t = 0; t < 9; ++t)
#pragma unroll
    for (int i = 0; i < 16; ++i) { float p = __builtin_amdgcn_exp2f(__builtin_fmaf(sc[t][i], cs, nmc)); sc[t][i] = p; sum += p; }
  sum += __shfl_xor(sum, 32);

  f32x16 o;
#pragma unroll
  for (int i = 0; i < 16; ++i) o[i] = 0.f;
  const char* vr = sV + c * AV_ROW + (wid * 32 + 4 * h) * 2;
#pragma unroll
  for (int t = 0; t < 9; ++t) {
    if (t >= tfirst) {
#pragma unroll
      for (int s = 0; s < 2; ++s) {
        const s16x4 lo = *reinterpret_cast<const s16x4*>(vr + (32 * t + 16 * s) * 2);
        const s16x4 hi = *reinterpret_cast<const s16x4*>(vr + (32 * t + 16 * s + 8) * 2);
        const bf16x8 vf = __builtin_shufflevector(lo, hi, 0, 1, 2, 3, 4, 5, 6, 7);
        u32x4 pk;
#pragma unroll
        for (int j = 0; j < 4; ++j) pk[j] = pack2(sc[t][8 * s + 2 * j], sc[t][8 * s + 2 * j + 1]);
        const bf16x8 pf = __builtin_bit_cast(bf16x8, pk);
        o = __builtin_amdgcn_mfma_f32_32x32x16_bf16(vf, pf, o, 0, 0, 0);
      }
    }
  }
  const float inv = 1.0f / sum;
  u16* op = a2 + (size_t)(q0 + c) * K2_ + head * 32 + 4 * h;
#pragma unroll
  for (int g = 0; g < 4; ++g) {
    u32x2 w; w[0] = pack2(o[4 * g] * inv, o[4 * g + 1] * inv); w[1] = pack2(o[4 * g + 2] * inv, o[4 * g + 3] * inv);
    *reinterpret_cast<u32x2*>(op + 8 * g) = w;
  }
}


__global__ __launch_bounds__(512, 2) void mid_kernel(
    const u16* __restrict__ xn, const u16* __restrict__ wg, const u16* __restrict__ wu,
    const u16* __restrict__ qk, const u16* __restrict__ vt, u16* __restrict__ o_a2) {
  extern __shared__ __attribute__((aligned(16))) char shm[];
  const int b = blockIdx.x;
  if (b >= 128) { attn_body(b - 128, qk, vt, o_a2, shm); return; }
  const int mt = (b >> 3) & 15, ntl = b & 7;
  const int brow = mt * 128, f0 = 2560 + ntl * 128;
  f32x4 acc[2][2][4][2];
#pragma unroll
  for (int c = 0; c < 2; ++c)
#pragma unroll
    for (int m = 0; m < 4; ++m)
#pragma unroll
      for (int n = 0; n < 2; ++n) { acc[0][c][m][n] = f32x4{0.f, 0.f, 0.f, 0.f}; acc[1][c][m][n] = f32x4{0.f, 0.f, 0.f, 0.f}; }
  big_mainloop<true, true>(xn + (size_t)brow * D_, wg + f0, wu + f0, F_, shm, acc);
  int T = threadIdx.x; asm volatile("" : "+v"(T));
  const int wid = T >> 6, lane = T & 63, wr = wid >> 2, wc = wid & 3, fr = lane & 15, fq = lane >> 4;
#pragma unroll
  for (int m = 0; m < 4; ++m) {
    const int row = brow + wr * 64 + m * 16 + fr;
#pragma unroll
    for (int n = 0; n < 2; ++n) {
      float hv[4];
#pragma unroll
      for (int j = 0; j < 4; ++j) {
        float g = rbf(acc[0][0][m][n][j]), u = rbf(acc[0][1][m][n][j]);
        hv[j] = (g > 0.f ? g : 0.f) * u;
      }
      u32x2 o; o[0] = pack2(hv[0], hv[1]); o[1] = pack2(hv[2], hv[3]);
      *reinterpret_cast<u32x2*>(o_a2 + (size_t)row * K2_ + D_ + f0 + wc * 32 + n * 16 + fq * 4) = o;
    }
  }
}

__global__ __launch_bounds__(256) void reduce_kernel(const u16* __restrict__ x, const float* __restrict__ part, u16* __restrict__ out) {
  const size_t i = ((size_t)blockIdx.x * 256 + threadIdx.x) * 8;
  const bf16x8 xv = *reinterpret_cast<const bf16x8*>(x + i);
  const f32x4 a0 = *reinterpret_cast<const f32x4*>(part + i), a1 = *reinterpret_cast<const f32x4*>(part + i + 4);
  const f32x4 b0 = *reinterpret_cast<const f32x4*>(part + (size_t)S_ * D_ + i), b1 = *reinterpret_cast<const f32x4*>(part + (size_t)S_ * D_ + i + 4);
  u32x4 o;
#pragma unroll
  for (int j = 0; j < 2; ++j) {
    o[j] = pack2(bf2f((u16)xv[2 * j]) + a0[2 * j] + b0[2 * j], bf2f((u16)xv[2 * j + 1]) + a0[2 * j + 1] + b0[2 * j + 1]);
    o[2 + j] = pack2(bf2f((u16)xv[4 + 2 * j]) + a1[2 * j] + b1[2 * j], bf2f((u16)xv[4 + 2 * j + 1]) + a1[2 * j + 1] + b1[2 * j + 1]);
  }
  *reinterpret_cast<u32x4*>(out + i) = o;
}

extern "C" void kernel_launch(void* const* d_in, const int* in_sizes, int n_in, void* d_out, int out_size, void* d_ws,
                              size_t ws_size, hipStream_t stream) {
  const u16* x = (const u16*)d_in[0];
  const u16* wqkv = (const u16*)d_in[1];
  const u16* wo = (const u16*)d_in[2];
  const u16* wg = (const u16*)d_in[4];
  const u16* wu = (const u16*)d_in[5];
  const u16* wd = (const u16*)d_in[6];
  char* ws = (char*)d_ws;
  u16* xn = (u16*)(ws);
  u16* qk = (u16*)(ws + (4u << 20));
  u16* vt = (u16*)(ws + (12u << 20));
  u16* a2 = (u16*)(ws + (16u << 20));
  float* part = (float*)(ws + (36u << 20));

  (void)hipFuncSetAttribute((const void*)gemm1_big_kernel, hipFuncAttributeMaxDynamicSharedMemorySize, BIG_LDS);
  (void)hipFuncSetAttribute((const void*)mid_kernel, hipFuncAttributeMaxDynamicSharedMemorySize, BIG_LDS);
  (void)hipFuncSetAttribute((const void*)gemm128_kernel<1>, hipFuncAttributeMaxDynamicSharedMemorySize, G_NS * G_STAGE);

  ln_kernel<<<S_ / 4, 256, 0, stream>>>(x, xn);
  gemm1_big_kernel<<<256, 512, BIG_LDS, stream>>>(xn, wqkv, wg, wu, qk, vt, a2);
  mid_kernel<<<384, 512, BIG_LDS, stream>>>(xn, wg, wu, qk, vt, a2);
  gemm128_kernel<1><<<256, 256, G_NS * G_STAGE, stream>>>(a2, wo, wd, nullptr, part);
  reduce_kernel<<<S_ * D_ / 8 / 256, 256, 0, stream>>>(x, part, (u16*)d_out);
}
```
